```python
import torch

def _softmax(x, axis=-1):
    return torch.softmax(x, dim=axis)
def _gelu(x):
    return torch.nn.functional.gelu(x, approximate="tanh")
def _silu(x):
    return torch.nn.functional.silu(x)
def _sigmoid(x):
    return torch.sigmoid(x)
def _layernorm(x, axis=-1, eps=1e-5):
    m = x.mean(dim=axis, keepdim=True)
    v = x.var(dim=axis, keepdim=True, unbiased=False)
    return (x - m) / torch.sqrt(v + eps)
def _scatter_add(tgt, idx, src, axis=0):
    return tgt.clone().index_add_(axis, idx, src)
def _segment_sum(src, seg, n):
    out = torch.zeros((n,) + src.shape[1:], dtype=src.dtype)
    return out.index_add_(0, seg, src)
def _pad(x, pw):
    flat = tuple(v for pair in reversed(pw) for v in pair)
    return torch.nn.functional.pad(x, flat)

kernel_name = 'standalone__gqa_block_cached__near_ridge'

def setup_inputs():

    _w = (lambda a: (lambda t: t.to(torch.bfloat16) if t.is_floating_point() else t)(torch.as_tensor(a)))
    B, Sq, D = 16, 64, 640
    Hq, Hkv, Sk, Dh = 32, 32, 512, 192
    assert Hq % Hkv == 0, (Hq, Hkv)
    return {
        "x": _w((lambda *s: torch.randn(*s, dtype=torch.bfloat16) * 0.8)(B, Sq, D)),
        "k_cache": _w((lambda *s: torch.randn(*s, dtype=torch.bfloat16) * 0.8)(B, Hkv, Sk, Dh)),
        "v_cache": _w((lambda *s: torch.randn(*s, dtype=torch.bfloat16) * 0.05)(B, Hkv, Sk, Dh)),
        "wq": _w((lambda *s: torch.randn(*s, dtype=torch.bfloat16) * 0.8)(D, Hq * Dh) * (D ** -0.5)),
        "wo": _w((lambda *s: torch.randn(*s, dtype=torch.bfloat16) * 0.05)(Hq * Dh, D) * ((Hq * Dh) ** -0.5)),
    }

def reference(x, k_cache, v_cache, wq, wo):
    B, Sq, D = x.shape
    Hq, Dh = 32, 192
    Hkv = k_cache.shape[1]
    rep = Hq // Hkv
    q = (lambda x, *d: x.permute(*d))((x @ wq).reshape(B, Sq, Hq, Dh), 0, 2, 1, 3)
    k = (lambda x, n, axis: torch.repeat_interleave(x, n, dim=axis))(k_cache, rep, axis=1)
    v = (lambda x, n, axis: torch.repeat_interleave(x, n, dim=axis))(v_cache, rep, axis=1)
    scores = torch.einsum("bhqd,bhkd->bhqk", q, k) * (Dh ** -0.5)
    p = _softmax(scores, axis=-1)
    o = torch.einsum("bhqk,bhkd->bhqd", p, v)
    return (lambda x, *d: x.permute(*d))(o, 0, 2, 1, 3).reshape(B, Sq, Hq * Dh) @ wo
```

```cpp
#include <hip/hip_runtime.h>
#include <hip/hip_bf16.h>
typedef unsigned short u16;
__device__ __forceinline__ float b2f(u16 v){ return __uint_as_float(((unsigned)v)<<16); }
__device__ __forceinline__ u16 f2b(float f){ __hip_bfloat16 b = __float2bfloat16(f); return *(u16*)&b; }
constexpr int B=16,SQ=64,D=640,H=32,SK=512,DH=192;
__global__ void qproj(const u16* x,const u16* wq,u16* q){
  int row=blockIdx.x;
  __shared__ float xs[D];
  for(int i=threadIdx.x;i<D;i+=256) xs[i]=b2f(x[row*D+i]);
  __syncthreads();
  for(int n=threadIdx.x;n<H*DH;n+=256){
    float a=0; for(int k=0;k<D;k++) a+=xs[k]*b2f(wq[(size_t)k*H*DH+n]);
    q[(size_t)row*H*DH+n]=f2b(a);
  }
}
__global__ void attn(const u16* q,const u16* k,const u16* v,u16* o){
  int row=blockIdx.x, h=blockIdx.y; int b=row/SQ;
  __shared__ float qs[DH]; __shared__ float p[SK]; __shared__ float red[256];
  for(int i=threadIdx.x;i<DH;i+=256) qs[i]=b2f(q[(size_t)row*H*DH+h*DH+i]);
  __syncthreads();
  const u16* kb=k+(size_t)(b*H+h)*SK*DH; const u16* vb=v+(size_t)(b*H+h)*SK*DH;
  float mx=-1e30f;
  for(int j=threadIdx.x;j<SK;j+=256){ float a=0; for(int d=0;d<DH;d++) a+=qs[d]*b2f(kb[j*DH+d]); a*=0.07216878364870323f; p[j]=a; mx=fmaxf(mx,a);}
  red[threadIdx.x]=mx; __syncthreads();
  for(int s=128;s>0;s>>=1){ if(threadIdx.x<s) red[threadIdx.x]=fmaxf(red[threadIdx.x],red[threadIdx.x+s]); __syncthreads(); }
  mx=red[0]; __syncthreads();
  float sm=0; for(int j=threadIdx.x;j<SK;j+=256){ float e=__expf(p[j]-mx); p[j]=e; sm+=e; }
  red[threadIdx.x]=sm; __syncthreads();
  for(int s=128;s>0;s>>=1){ if(threadIdx.x<s) red[threadIdx.x]+=red[threadIdx.x+s]; __syncthreads(); }
  sm=red[0];
  for(int d=threadIdx.x;d<DH;d+=256){ float a=0; for(int j=0;j<SK;j++) a+=p[j]*b2f(vb[j*DH+d]); o[(size_t)row*H*DH+h*DH+d]=f2b(a/sm);}
}
__global__ void oproj(const u16* o,const u16* wo,u16* out){
  int row=blockIdx.x;
  __shared__ float xs[H*DH];
  for(int i=threadIdx.x;i<H*DH;i+=256) xs[i]=b2f(o[(size_t)row*H*DH+i]);
  __syncthreads();
  for(int n=threadIdx.x;n<D;n+=256){
    float a=0; for(int k=0;k<H*DH;k++) a+=xs[k]*b2f(wo[(size_t)k*D+n]);
    out[(size_t)row*D+n]=f2b(a);
  }
}
extern "C" void kernel_launch(void* const* d_in, const int* in_sizes, int n_in, void* d_out, int out_size, void* d_ws, size_t ws_size, hipStream_t stream){
  const u16* x=(const u16*)d_in[0]; const u16* k=(const u16*)d_in[1]; const u16* v=(const u16*)d_in[2]; const u16* wq=(const u16*)d_in[3]; const u16* wo=(const u16*)d_in[4];
  u16* q=(u16*)d_ws; u16* o=q+(size_t)1024*6144;
  qproj<<<1024,256,0,stream>>>(x,wq,q);
  attn<<<dim3(1024,32),256,0,stream>>>(q,k,v,o);
  oproj<<<1024,256,0,stream>>>(o,wo,(u16*)d_out);
}
```

```cpp
#include <hip/hip_runtime.h>
#include <stdint.h>

#define LAS __attribute__((address_space(3)))
#define DI __device__ __forceinline__
typedef short s16x4 __attribute__((ext_vector_type(4)));
typedef short s16x8 __attribute__((ext_vector_type(8)));
typedef __bf16 bf16x8 __attribute__((ext_vector_type(8)));
typedef __bf16 bf16x2 __attribute__((ext_vector_type(2)));
typedef float f32x2 __attribute__((ext_vector_type(2)));
typedef float f32x4 __attribute__((ext_vector_type(4)));
typedef unsigned u32x4 __attribute__((ext_vector_type(4)));
typedef unsigned u32x2 __attribute__((ext_vector_type(2)));

#define POL_KV " nt"
#define POL_W ""
#define X_NT 0

namespace {
constexpr int NB = 16, SQ = 64, DM = 640, NH = 32, SK = 512, DH = 192;
constexpr int KVT = 24576;
constexpr int WT = 12288;
constexpr int LDS_KV = 0, LDS_W = 4 * KVT;
constexpr int LDS_BYTES = LDS_W + 5 * WT;
constexpr int XCH1 = LDS_W + 3 * WT;
constexpr int XCH2 = LDS_KV;
constexpr int T_MAIN = 20, T_TAIL = 52, T_END = 62;
constexpr float SL2E = 0.07216878364870323f * 1.4426950408889634f;

struct Sched {
  int consW[80];
  int issW[80];
  int locW[80];
  int kvp[T_MAIN];
  int waitP[T_END];
  int waitA[T_END];
};
constexpr Sched make_sched() {
  Sched s{};
  for (int m = 0; m < 20; ++m) s.consW[m] = m;
  { int m = 20;
    for (int c = 0; c < 8; ++c) {
      if (c < 6) s.consW[m++] = T_MAIN + 2 * c;
      if (c < 7) { s.consW[m++] = T_MAIN + 2 * c + 1; s.consW[m++] = T_MAIN + 2 * c + 1; }
    } }
  for (int m = 40; m < 60; ++m) s.consW[m] = T_MAIN + 17 + (m - 40) / 2;
  for (int m = 60; m < 80; ++m) s.consW[m] = T_TAIL + (m - 60) / 2;
  for (int m = 0; m < 65; ++m) { s.locW[m] = LDS_W + (m % 5) * WT; s.issW[m] = m < 5 ? -1 : s.consW[m - 5] + 1; }
  s.issW[43] = s.issW[44] = T_MAIN + 17;
  s.locW[65] = LDS_KV + 1 * KVT; s.locW[66] = s.locW[65] + WT; s.issW[65] = s.issW[66] = T_MAIN + 30;
  s.locW[67] = LDS_KV + 2 * KVT; s.locW[68] = s.locW[67] + WT; s.issW[67] = s.issW[68] = T_MAIN + 31;
  s.locW[69] = LDS_KV + 3 * KVT; s.locW[70] = s.locW[69] + WT; s.issW[69] = s.issW[70] = T_TAIL;
  s.locW[71] = LDS_KV + 0 * KVT; s.locW[72] = s.locW[71] + WT; s.issW[71] = s.issW[72] = T_TAIL + 1;
  for (int m = 73; m < 80; ++m) { s.locW[m] = s.locW[m - 13]; s.issW[m] = s.consW[m - 13] + 1; }
  for (int t = 0; t < T_MAIN; ++t) s.kvp[t] = (t >= 4 && t < 16) ? 2 : 0;
  int endW[80] = {}, endKV[32] = {};
  int posP = 0, posA = 0, kvpos = 0;
  for (int m = 0; m < 5; ++m) { posP += 3; endW[m] = posP; }
  for (int t = 0; t < T_END; ++t) {
    int needP = -1, needA = -1;
    for (int m = 0; m < 80; ++m) if (s.consW[m] == t) {
      if (s.issW[m] < T_TAIL) { if (endW[m] > needP) needP = endW[m]; } else { if (endW[m] > needA) needA = endW[m]; }
    }
    if (t >= T_MAIN && t < T_TAIL && endKV[t - T_MAIN] > needP) needP = endKV[t - T_MAIN];
    s.waitP[t] = needP < 0 ? -1 : posP - needP;
    s.waitA[t] = needA < 0 ? -1 : posA - needA;
    if (t == T_TAIL) s.waitP[t] = 0;
    if (t > T_TAIL) s.waitP[t] = -1;
    for (int m = 5; m < 80; ++m) if (s.issW[m] == t) {
      if (t < T_TAIL) { posP += 3; endW[m] = posP; } else { posA += 3; endW[m] = posA; }
    }
    if (t < T_MAIN) { for (int i = 0; i < s.kvp[t]; ++i) { ++posP; ++kvpos; if (kvpos % 6 == 0) endKV[kvpos / 6 - 1] = posP; } }
    else if (t - T_MAIN >= 1 && t - T_MAIN + 3 < 32) { posP += 6; endKV[t - T_MAIN + 3] = posP; }
  }
  return s;
}
constexpr Sched SC = make_sched();
constexpr int kvp_before(int t) { int n = 0; for (int i = 0; i < t; ++i) n += SC.kvp[i]; return n; }
static_assert(kvp_before(T_MAIN) == 24, "start-up must issue four K/V tiles");
constexpr bool sched_ok() {
  for (int t = 0; t < T_END; ++t) if (SC.waitP[t] > 63 || SC.waitA[t] > 63) return false;
  for (int m = 0; m < 80; ++m) if (SC.issW[m] >= SC.consW[m]) return false;
  return true;
}
static_assert(sched_ok(), "schedule");

template <int N> struct IC { static constexpr int v = N; };
template <int I, int N, class F> DI void static_for(F&& f) { if constexpr (I < N) { f(IC<I>{}); static_for<I + 1, N>(f); } }

DI unsigned pk2(float a, float b) { f32x2 v = {a, b}; return __builtin_bit_cast(unsigned, __builtin_convertvector(v, bf16x2)); }
DI bf16x8 as_frag(u32x4 v) { return __builtin_bit_cast(bf16x8, v); }
DI f32x4 mfma(bf16x8 a, bf16x8 b, f32x4 c) { return __builtin_amdgcn_mfma_f32_16x16x32_bf16(a, b, c, 0, 0, 0); }
DI bf16x8 tr_frag(LAS char* lo, LAS char* hi) {
  s16x4 a = __builtin_amdgcn_ds_read_tr16_b64_v4i16((LAS s16x4*)lo);
  s16x4 b = __builtin_amdgcn_ds_read_tr16_b64_v4i16((LAS s16x4*)hi);
  return __builtin_bit_cast(bf16x8, __builtin_shufflevector(a, b, 0, 1, 2, 3, 4, 5, 6, 7));
}
DI LAS char* rebase(LAS char* p, int off) {
  unsigned a = (unsigned)(uintptr_t)p + off;
  asm volatile("" : "+v"(a));
  return (LAS char*)(uintptr_t)a;
}
DI int swz(int k) { return ((k & 3) << 1) ^ ((k >> 2) * 5); }
DI float xmax(float v) {
  auto r = __builtin_amdgcn_permlane32_swap(__float_as_uint(v), __float_as_uint(v), false, false);
  v = fmaxf(__uint_as_float(r[0]), __uint_as_float(r[1]));
  auto q = __builtin_amdgcn_permlane16_swap(__float_as_uint(v), __float_as_uint(v), false, false);
  return fmaxf(__uint_as_float(q[0]), __uint_as_float(q[1]));
}
DI float xsum(float v) {
  auto r = __builtin_amdgcn_permlane32_swap(__float_as_uint(v), __float_as_uint(v), false, false);
  v = __uint_as_float(r[0]) + __uint_as_float(r[1]);
  auto q = __builtin_amdgcn_permlane16_swap(__float_as_uint(v), __float_as_uint(v), false, false);
  return __uint_as_float(q[0]) + __uint_as_float(q[1]);
}
template <int N> DI void bar_v() {
  if constexpr (N >= 0) asm volatile("s_waitcnt vmcnt(%0) lgkmcnt(0)\n\ts_barrier" ::"i"(N) : "memory");
  else asm volatile("s_waitcnt lgkmcnt(0)\n\ts_barrier" ::: "memory");
}
typedef __amdgpu_buffer_rsrc_t srd_t;
#define DMA3_ASM(POL)                                                                                          \
  asm volatile("s_mov_b32 m0, %0\n\ts_nop 4\n\t"                                                               \
               "buffer_load_dwordx4 %3, %1, %2 offen" POL " lds\n\t"                                           \
               "buffer_load_dwordx4 %4, %1, %2 offen offset:1024" POL " lds\n\t"                               \
               "buffer_load_dwordx4 %5, %1, %2 offen offset:2048" POL " lds" ::"s"(m0v),                       \
               "s"(srd), "s"(soff), "v"(v0), "v"(v1), "v"(v2) : "memory")
#define DMA1_ASM(POL)                                                                                          \
  asm volatile("s_mov_b32 m0, %0\n\ts_nop 4\n\t"                                                               \
               "buffer_load_dwordx4 %3, %1, %2 offen" POL " lds" ::"s"(m0v), "s"(srd), "s"(soff), "v"(v0) : "memory")
DI void dma3_kv(unsigned m0v, srd_t srd, unsigned soff, int v0, int v1, int v2) { DMA3_ASM(POL_KV); }
DI void dma3_w(unsigned m0v, srd_t srd, unsigned soff, int v0, int v1, int v2) { DMA3_ASM(POL_W); }
DI void dma1_kv(unsigned m0v, srd_t srd, unsigned soff, int v0) { DMA1_ASM(POL_KV); }

#define STAMP(i) do {} while (0)

DI void qstep(f32x4 (&acc)[12], LAS char* const (&tb)[4], int loc, bf16x8 xf) {
  LAS char* const t4[4] = {rebase(tb[0], loc), rebase(tb[1], loc), rebase(tb[2], loc), rebase(tb[3], loc)};
#pragma unroll
  for (int dt = 0; dt < 12; ++dt) {
    LAS char* p = t4[dt & 3] + (dt >> 2) * 128;
    acc[dt] = mfma(tr_frag(p, p + 6144), xf, acc[dt]);
  }
}
DI void q_fix(const f32x4 (&acc)[12], bf16x8 (&q)[6]) {
#pragma unroll
  for (int s = 0; s < 6; ++s) {
    unsigned x0 = pk2(acc[2 * s][0], acc[2 * s][1]), x1 = pk2(acc[2 * s][2], acc[2 * s][3]);
    unsigned y0 = pk2(acc[2 * s + 1][0], acc[2 * s + 1][1]), y1 = pk2(acc[2 * s + 1][2], acc[2 * s + 1][3]);
    auto a0 = __builtin_amdgcn_permlane32_swap(x0, y0, false, false);
    auto a1 = __builtin_amdgcn_permlane32_swap(x1, y1, false, false);
    auto b0 = __builtin_amdgcn_permlane16_swap(a0[0], a0[1], false, false);
    auto b1 = __builtin_amdgcn_permlane16_swap(a1[0], a1[1], false, false);
    q[s] = as_frag(u32x4{b0[0], b1[0], b0[1], b1[1]});
  }
}
DI u32x4 ldx(const u32x4* p) {
  return *p;
}

__global__ __launch_bounds__(512, 2) void fused_attn(const uint16_t* __restrict__ x, const uint16_t* __restrict__ kc,
                                                     const uint16_t* __restrict__ vc, const uint16_t* __restrict__ wq,
                                                     const uint16_t* __restrict__ wo, uint16_t* __restrict__ part
) {
  __shared__ __attribute__((aligned(1024))) char smem[LDS_BYTES];
  LAS char* L = (LAS char*)smem;
  const unsigned Lb = (unsigned)(uintptr_t)L;
  const int wave = __builtin_amdgcn_readfirstlane(threadIdx.x >> 6), lane = threadIdx.x & 63;
  const int w = wave & 3;
  const int xcd = blockIdx.x & 7, idx = blockIdx.x >> 3, b = idx >> 1, hp = idx & 1;
  const int h1 = xcd * 4 + hp * 2;
  const int rot = (b * 5) >> 2;
  const int g = lane >> 4;
  STAMP(0);

  const srd_t srdWo = __builtin_amdgcn_make_buffer_rsrc((void*)wo, 0, NH * DH * DM * 2, 0x00020000);
  int vWo[3];
#pragma unroll
  for (int i = 0; i < 3; ++i) {
    const int r = 24 * w + 8 * i + (lane >> 3), c = lane & 7;
    vWo[i] = r * 1280 + ((c ^ (((r >> 1) & 3) << 1)) << 4) - 1024 * i;
  }
  auto issue_wo = [&](int m, int loc) {
    const int hh = h1 + m / 20, cg = (m % 20) >> 1, hk = m & 1;
    const unsigned soff = (hh * DH + hk * 96) * 1280 + cg * 128;
    dma3_w(Lb + loc + w * 3072, srdWo, soff, vWo[0], vWo[1], vWo[2]);
  };

  if (wave >= 4) {
    const srd_t srdK = __builtin_amdgcn_make_buffer_rsrc((void*)kc, 0, NB * NH * SK * DH * 2, 0x00020000);
    const srd_t srdV = __builtin_amdgcn_make_buffer_rsrc((void*)vc, 0, NB * NH * SK * DH * 2, 0x00020000);
    const srd_t srdWq = __builtin_amdgcn_make_buffer_rsrc((void*)wq, 0, DM * NH * DH * 2, 0x00020000);
    const srd_t srdP = __builtin_amdgcn_make_buffer_rsrc((void*)part, 0, NB * 16 * SQ * DM * 2, 0x00020000);
    int vKV[6], vWq[3];
#pragma unroll
    for (int i = 0; i < 6; ++i) {
      const int p = 64 * i + lane, r = p / 24;
      vKV[i] = ((16 * lane) ^ (swz(r >> 1) << 4)) + (i >= 3 ? 3072 : 0);
    }
#pragma unroll
    for (int i = 0; i < 3; ++i) {
      const int p = 192 * w + 64 * i + lane, r = p / 24, c = p - 24 * r;
      const int kr = 8 * ((r >> 2) & 3) + 4 * (r >> 4) + (r & 3);
      vWq[i] = kr * 12288 + ((c ^ swz((r >> 1) & 7)) << 4) - 1024 * i;
    }
    const unsigned kv0 = (unsigned)(b * NH + h1) * (SK * DH * 2) + w * 6144;
    auto issue_w = [&](auto M) {
      constexpr int m = decltype(M)::v;
      if constexpr (m < 40) {
        int kt = m % 20 + rot; kt -= kt >= 20 ? 20 : 0;
        const unsigned soff = kt * (32 * 12288) + (h1 + m / 20) * 384;
        dma3_w(Lb + SC.locW[m] + w * 3072, srdWq, soff, vWq[0], vWq[1], vWq[2]);
      } else issue_wo(m - 40, SC.locW[m]);
    };
    auto kv_soff = [&](int n) -> unsigned { return kv0 + (n >> 4) * (SK * DH * 2) + ((n & 15) >> 1) * KVT; };
    auto issue_kv = [&](auto N) {
      constexpr int n = decltype(N)::v;
      const unsigned soff = kv_soff(n), m0v = Lb + LDS_KV + (n & 3) * KVT + w * 6144;
      if constexpr (n & 1) { dma3_kv(m0v, srdV, soff, vKV[0], vKV[1], vKV[2]); dma3_kv(m0v + 3072, srdV, soff, vKV[3], vKV[4], vKV[5]); }
      else { dma3_kv(m0v, srdK, soff, vKV[0], vKV[1], vKV[2]); dma3_kv(m0v + 3072, srdK, soff, vKV[3], vKV[4], vKV[5]); }
    };
    auto issue_kv_piece = [&](auto J) {
      constexpr int n = decltype(J)::v / 6, i = decltype(J)::v % 6;
      const unsigned soff = kv_soff(n) + (i % 3) * 1024, m0v = Lb + LDS_KV + n * KVT + w * 6144 + i * 1024;
      if constexpr (n & 1) dma1_kv(m0v, srdV, soff, vKV[i]); else dma1_kv(m0v, srdK, soff, vKV[i]);
    };
    LAS char* ob[4];
    { const int row = 4 * g + ((lane & 15) >> 2), p = lane & 3, mm = ((row >> 1) & 3) << 1;
#pragma unroll
      for (int e = 0; e < 4; ++e) ob[e] = L + row * 128 + ((((2 * e) ^ mm) | (p >> 1)) << 4) + 8 * (p & 1); }
    f32x4 acc[40];
#pragma unroll
    for (int i = 0; i < 40; ++i) acc[i] = f32x4{0.f, 0.f, 0.f, 0.f};
    bf16x8 of[6];
    const unsigned pbase = (unsigned)((b * 16 + xcd * 2 + hp) * 10) * 8192 + w * 2048 + lane * 16;

    static_for<0, 5>([&](auto M) { issue_w(M); });
    static_for<0, T_END>([&](auto T) {
      constexpr int t = decltype(T)::v;
      bar_v<SC.waitP[t]>();
      if constexpr (t == T_MAIN || t == T_MAIN + 16 || t == T_TAIL) STAMP(t == T_MAIN ? 1 : t == T_TAIL ? 3 : 2);
      if constexpr (t < T_TAIL) {
        static_for<5, 80>([&](auto M) { if constexpr (SC.issW[decltype(M)::v] == t) issue_w(M); });
        if constexpr (t < T_MAIN) static_for<kvp_before(t), kvp_before(t + 1)>([&](auto J) { issue_kv_piece(J); });
        else if constexpr (t - T_MAIN >= 1 && t - T_MAIN + 3 < 32) issue_kv(IC<t - T_MAIN + 3>{});
      }
      if constexpr (t == T_MAIN + 16 || t == T_TAIL) {
        LAS char* const xb = rebase(L, (t == T_TAIL ? XCH2 : XCH1) + w * 6144 + lane * 16);
#pragma unroll
        for (int s = 0; s < 6; ++s) of[s] = *(LAS bf16x8*)(xb + s * 1024);
      }
      constexpr int cg = t >= T_TAIL ? t - T_TAIL : t - (T_MAIN + 17);
      if constexpr (cg >= 0 && cg < 10) {
        constexpr int m = (t >= T_TAIL ? 60 : 40) + 2 * cg;
#pragma unroll
        for (int hk = 0; hk < 2; ++hk) {
          const int loc = SC.locW[m + hk];
          LAS char* const o4[4] = {rebase(ob[0], loc), rebase(ob[1], loc), rebase(ob[2], loc), rebase(ob[3], loc)};
#pragma unroll
          for (int ks = 0; ks < 3; ++ks)
#pragma unroll
            for (int e = 0; e < 4; ++e) {
              LAS char* p = o4[e] + ks * 4096;
              acc[4 * cg + e] = mfma(tr_frag(p, p + 2048), of[3 * hk + ks], acc[4 * cg + e]);
            }
        }
        if constexpr (t >= T_TAIL) {
#pragma unroll
          for (int hf = 0; hf < 2; ++hf) {
            const f32x4 a = acc[4 * cg + 2 * hf], c = acc[4 * cg + 2 * hf + 1];
            const u32x4 v = {pk2(a[0], a[1]), pk2(a[2], a[3]), pk2(c[0], c[1]), pk2(c[2], c[3])};
            __builtin_amdgcn_raw_buffer_store_b128(v, srdP, pbase + cg * 8192 + hf * 1024, 0, 16);
          }
        }
      }
    });
    STAMP(4);
    asm volatile("s_waitcnt vmcnt(0)" ::: "memory");
    STAMP(5);
  } else {
    const int r16 = lane & 15;
    const u32x4* xp = (const u32x4*)(x + (size_t)(b * SQ + w * 16 + r16) * DM + 8 * g);
    LAS char* tb[4];
    { const int row = 4 * g + (r16 >> 2), p = lane & 3, mm = swz(row >> 1);
#pragma unroll
      for (int e = 0; e < 4; ++e) tb[e] = L + row * 384 + ((((2 * e) ^ (mm & 6)) | ((p >> 1) ^ (mm & 1))) << 4) + 8 * (p & 1); }
    LAS char* kb[2];
    { const int mm = swz(r16 >> 1);
      kb[0] = L + r16 * 384 + ((g ^ mm) << 4); kb[1] = L + r16 * 384 + (((4 + g) ^ mm) << 4); }
    auto xload = [&](int j) -> bf16x8 { int kt = j + rot; kt -= kt >= 20 ? 20 : 0; return as_frag(ldx(xp + 4 * kt)); };

    bf16x8 q[6];
    f32x4 qa[12];
    {
      bf16x8 xf[20];
#pragma unroll
      for (int j = 0; j < 20; ++j) xf[j] = xload(j);
#pragma unroll
      for (int i = 0; i < 12; ++i) qa[i] = f32x4{0.f, 0.f, 0.f, 0.f};
#pragma unroll
      for (int j = 0; j < 20; ++j) { bar_v<-1>(); qstep(qa, tb, LDS_W + (j % 5) * WT, xf[j]); }
      q_fix(qa, q);
#pragma unroll
      for (int i = 0; i < 12; ++i) qa[i] = f32x4{0.f, 0.f, 0.f, 0.f};
    }
    bf16x8 xn[3] = {xload(0), xload(1), xload(2)};
    int xj = 3, wslot = 0;
    auto next_w = [&]() -> int { const int loc = LDS_W + wslot * WT; wslot = wslot == 4 ? 0 : wslot + 1; return loc; };

#pragma nounroll
    for (int p = 0; p < 2; ++p) {
      float mrun = -__builtin_inff(), lsum = 0.f;
      f32x4 o[12];
#pragma unroll
      for (int i = 0; i < 12; ++i) o[i] = f32x4{0.f, 0.f, 0.f, 0.f};
#pragma nounroll
      for (int c = 0; c < 8; ++c) {
        const int ks = LDS_KV + (c & 1) * 2 * KVT, vs = ks + KVT;
        const bool q1 = p == 0 && c < 6, q2 = p == 0 && c < 7;
        const bf16x8 x0 = xn[0], x1 = xn[1], x2 = xn[2];
        bar_v<-1>();
        if (q1) {
          if (c < 5) { xn[0] = xload(xj); xn[1] = xload(xj + 1); xn[2] = xload(xj + 2); xj += 3; }
          else { xn[1] = xload(18); xn[2] = xload(19); }
        }
        f32x4 s[4];
        LAS char* const k2[2] = {rebase(kb[0], ks), rebase(kb[1], ks)};
#pragma unroll
        for (int kt = 0; kt < 4; ++kt) {
          s[kt] = f32x4{0.f, 0.f, 0.f, 0.f};
#pragma unroll
          for (int d = 0; d < 6; ++d)
            s[kt] = mfma(*(LAS bf16x8*)(k2[d & 1] + kt * 6144 + (d >> 1) * 128), q[d], s[kt]);
        }
        if (q1) qstep(qa, tb, next_w(), x0);
        float mx = fmaxf(fmaxf(s[0][0], s[0][1]), fmaxf(s[0][2], s[0][3]));
#pragma unroll
        for (int kt = 1; kt < 4; ++kt) mx = fmaxf(mx, fmaxf(fmaxf(s[kt][0], s[kt][1]), fmaxf(s[kt][2], s[kt][3])));
        const float mnew = fmaxf(mrun, xmax(mx));
        const float alpha = __builtin_amdgcn_exp2f((mrun - mnew) * SL2E), mb = -mnew * SL2E;
        mrun = mnew;
        float ps = 0.f;
#pragma unroll
        for (int kt = 0; kt < 4; ++kt)
#pragma unroll
          for (int r = 0; r < 4; ++r) { s[kt][r] = __builtin_amdgcn_exp2f(fmaf(s[kt][r], SL2E, mb)); ps += s[kt][r]; }
        lsum = lsum * alpha + ps;
        bf16x8 pf[2];
#pragma unroll
        for (int j = 0; j < 2; ++j)
          pf[j] = as_frag(u32x4{pk2(s[2 * j][0], s[2 * j][1]), pk2(s[2 * j][2], s[2 * j][3]),
                                pk2(s[2 * j + 1][0], s[2 * j + 1][1]), pk2(s[2 * j + 1][2], s[2 * j + 1][3])});
#pragma unroll
        for (int i = 0; i < 12; ++i) o[i] *= alpha;
        bar_v<-1>();
        qstep(o, tb, vs, pf[0]);
        qstep(o, tb, vs + 12288, pf[1]);
        if (q2) { qstep(qa, tb, next_w(), x1); qstep(qa, tb, next_w(), x2); }
      }
      const float inv = 1.f / xsum(lsum);
      const int xo = (p == 0 ? XCH1 : XCH2) + w * 6144 + lane * 16;
#pragma unroll
      for (int s2 = 0; s2 < 6; ++s2) {
        const f32x4 a = o[2 * s2] * inv, c2 = o[2 * s2 + 1] * inv;
        *(LAS u32x4*)(L + xo + s2 * 1024) = u32x4{pk2(a[0], a[1]), pk2(a[2], a[3]), pk2(c2[0], c2[1]), pk2(c2[2], c2[3])};
      }
      if (p == 0) q_fix(qa, q);
    }
    static_for<T_TAIL, T_END>([&](auto T) {
      constexpr int t = decltype(T)::v;
      bar_v<SC.waitA[t]>();
      static_for<60, 80>([&](auto M) { constexpr int m = decltype(M)::v; if constexpr (SC.issW[m] == t) issue_wo(m - 40, SC.locW[m]); });
    });
    STAMP(4);
  }
}

__global__ __launch_bounds__(320) void reduce_out(const u32x4* __restrict__ part, uint16_t* __restrict__ out) {
  const int i = blockIdx.x * 320 + threadIdx.x;
  const int b = i / 5120, r = i - b * 5120;
  const u32x4* p = part + (size_t)b * 16 * 5120 + r;
  u32x4 v[16];
#pragma unroll
  for (int j = 0; j < 16; ++j) v[j] = p[j * 5120];
  float s[8] = {};
#pragma unroll
  for (int j = 0; j < 16; ++j)
#pragma unroll
    for (int k = 0; k < 4; ++k) { s[2 * k] += __uint_as_float(v[j][k] << 16); s[2 * k + 1] += __uint_as_float(v[j][k] & 0xffff0000u); }
  const int lane = r & 63, hf = (r >> 6) & 1, w = (r >> 7) & 3, cg = r >> 9;
  uint16_t* o = out + (size_t)(b * SQ + w * 16 + (lane & 15)) * DM + cg * 64 + hf * 32 + 4 * (lane >> 4);
  *(u32x2*)o = u32x2{pk2(s[0], s[1]), pk2(s[2], s[3])};
  *(u32x2*)(o + 16) = u32x2{pk2(s[4], s[5]), pk2(s[6], s[7])};
}
}

extern "C" void kernel_launch(void* const* d_in, const int* in_sizes, int n_in, void* d_out, int out_size, void* d_ws,
                              size_t ws_size, hipStream_t stream) {
  const uint16_t* x = (const uint16_t*)d_in[0];
  const uint16_t* kc = (const uint16_t*)d_in[1];
  const uint16_t* vc = (const uint16_t*)d_in[2];
  const uint16_t* wq = (const uint16_t*)d_in[3];
  const uint16_t* wo = (const uint16_t*)d_in[4];
  uint16_t* part = (uint16_t*)d_ws;
  fused_attn<<<256, 512, 0, stream>>>(x, kc, vc, wq, wo, part);
  reduce_out<<<256, 320, 0, stream>>>((const u32x4*)part, (uint16_t*)d_out);
}
```

```cpp
#include <hip/hip_runtime.h>
#include <stdint.h>

#define LAS __attribute__((address_space(3)))
#define GAS __attribute__((address_space(1)))
#define DI __device__ __forceinline__
typedef short s16x4 __attribute__((ext_vector_type(4)));
typedef short s16x8 __attribute__((ext_vector_type(8)));
typedef __bf16 bf16x8 __attribute__((ext_vector_type(8)));
typedef __bf16 bf16x2 __attribute__((ext_vector_type(2)));
typedef float f32x2 __attribute__((ext_vector_type(2)));
typedef float f32x4 __attribute__((ext_vector_type(4)));
typedef float f32x16 __attribute__((ext_vector_type(16)));
typedef unsigned u32x4 __attribute__((ext_vector_type(4)));
typedef unsigned u32x2 __attribute__((ext_vector_type(2)));

#define POL_KV " nt"
#define POL_W ""
#define CLASS_EXPR (b & 1)
#define POL_X ""
#define POL_WARM ""
#define CODE_PF 39424
#define ILV 1
#define PRIO_P 0

namespace {
constexpr int NB = 16, SQ = 64, DM = 640, NH = 32, SK = 512, DH = 192;
constexpr int KVT = 24576;
constexpr int WT = 12288;
constexpr int LDS_KV = 0, LDS_W = 4 * KVT;
constexpr int LDS_BYTES = LDS_W + 5 * WT;
constexpr int WARM_LDS = LDS_BYTES;
constexpr int MS_LDS = LDS_BYTES + 2560;
constexpr int ML_LDS = LDS_BYTES + 3072;
constexpr int T_MAIN = 20, T_TAIL = 52, T_END = 62;
constexpr float SL2E = 0.07216878364870323f * 1.4426950408889634f;

struct Sched {
  int consW[80];
  int issW[80];
  int locW[80];
  int kv3;
  int xq[2], xch1[2], xp1;
  int issKV[2][4];
  int waitP[2][T_END];
  int waitA[T_END];
};
#define KVA 0, 3, 7, 20
#define KVB 15, 17, 19, 20
#define A_PACE 1
#define SDEFER 3
#define TBARS 0x3ff
constexpr bool tbar(int g) { return g >= 0 && g < 10 && ((TBARS >> g) & 1); }
constexpr int tbar_up(int g) { while (g < 10 && !tbar(g)) ++g; return g; }
constexpr int tbar_dn(int g) { while (g > 0 && !tbar(g)) --g; return g; }
#define LAZY_THR 8
#define PATQ 0, 2, 1, 2, 2, 1, 2, 2, 1, 2, 2, 1, 2, 0
#define PATO 1, 2, 1, 2, 1, 2, 1, 2, 2, 1, 2, 2, 1, 0
#define W7END 19
#define LEAD_SW 99
constexpr int KVA_[4] = {KVA};
constexpr int kv_at(int t) {
  for (int n = KVA_[3] < T_MAIN ? 4 : 3; n < 32; ++n) if (T_MAIN + n - (n >= LEAD_SW ? 2 : 3) == t) return n;
  return -1;
}
#define HOLD 7
#define HOLD0 3
#define WCT -1, 19
#define WCN 0, 15
constexpr int WARMC_T[2] = {WCT}, WARMC_N[2] = {WCN};
constexpr Sched make_sched() {
  Sched s{};
  for (int m = 0; m < 20; ++m) s.consW[m] = m;
  for (int ph = 0; ph < 2; ++ph) {
    int m = 20 + 20 * ph;
    const int pat[2][14] = {{PATQ}, {PATO}};
    for (int n = 0; n < 14; ++n)
      for (int i = 0; i < pat[ph][n]; ++i) s.consW[m++] = T_MAIN + n + 17 * ph;
  }
  for (int m = 60; m < 80; ++m) s.consW[m] = T_TAIL + (m - 60) / 2;
  int freeAt[13] = {T_TAIL + tbar_up(1), T_TAIL + tbar_up(1), T_TAIL, T_MAIN + 30, T_MAIN + 31, T_MAIN + 31, 1, 1, -1, -1, -1, -1, 1};
  for (int m = 0; m < 80; ++m) {
    if (m == W7END) { s.kv3 = freeAt[6] > freeAt[7] ? freeAt[6] : freeAt[7]; freeAt[6] = freeAt[7] = T_TAIL; }
    if (m == 40) {
      s.xq[0] = 8 * WT; s.xq[1] = 9 * WT; freeAt[8] = freeAt[9] = T_MAIN + 15;
      s.xp1 = 10 * WT; freeAt[10] = T_MAIN + 16;
      s.xch1[0] = 11 * WT; s.xch1[1] = 12 * WT; freeAt[11] = freeAt[12] = T_MAIN + 17;
    }
    const int lo = m >= 60 ? 0 : (m >= 5 && m < W7END) ? 6 : 8;
    int u = lo;
    for (int v = lo; v < 13; ++v) if (freeAt[v] < freeAt[u]) u = v;
    s.locW[m] = u * WT; s.issW[m] = freeAt[u]; freeAt[u] = m >= 60 ? T_TAIL + tbar_up(s.consW[m] + 1 - T_TAIL) : s.consW[m] + 1;
    if (m >= 20 && m < 40 && s.issW[m] < T_MAIN) s.issW[m] = m < 22 ? T_MAIN - 1 : T_MAIN;
  }
  const int kva[4] = {KVA}, kvb[4] = {KVB};
  for (int i = 0; i < 4; ++i) { s.issKV[0][i] = kva[i]; s.issKV[1][i] = kvb[i]; }
  for (int cl = 0; cl < 2; ++cl) {
    int endW[80] = {}, endKV[32] = {};
    int posP = 10, posA = 0;
    for (int m = 0; m < 4; ++m) { posP += 3; endW[m] = posP; }
    for (int t = 0; t < T_END; ++t) {
      int needP = -1, needA = -1;
      const bool ahead = t < T_MAIN - 1 || t >= T_TAIL;
      for (int m = 0; m < 80; ++m) if (m >= 60 ? T_TAIL + tbar_dn(s.consW[m] - T_TAIL - 1) == t : s.consW[m] == t || (ahead && s.consW[m] == t + 1)) {
        if (s.issW[m] < T_TAIL) { if (endW[m] > needP) needP = endW[m]; } else { if (endW[m] > needA) needA = endW[m]; }
      }
      if (t >= T_MAIN && t < T_TAIL && endKV[t - T_MAIN] > needP) needP = endKV[t - T_MAIN];
      if (t == T_TAIL - 1 && endW[60] > needP) needP = endW[60];
      s.waitP[cl][t] = needP < 0 ? -1 : posP - needP > 60 ? 60 : posP - needP;
      s.waitA[t] = needA < 0 ? -1 : posA - needA;
      if (t == T_TAIL) s.waitP[cl][t] = 0;
      if (t > T_TAIL) s.waitP[cl][t] = -1;
      const bool kv_first = cl == 0 && t == HOLD;
      if (kv_first) for (int n = 0; n < 4; ++n) if (s.issKV[cl][n] == t) { posP += 6; endKV[n] = posP; }
      for (int m = 4; m < 80; ++m) if ((cl == 0 && s.issW[m] > HOLD0 && s.issW[m] < HOLD ? HOLD : s.issW[m]) == t) {
        if (t < T_TAIL) { posP += 3; endW[m] = posP; } else { posA += 3; endW[m] = posA; }
      }
      if (t < T_MAIN) { if (!kv_first) for (int n = 0; n < 4; ++n) if (s.issKV[cl][n] == t) { posP += 6; endKV[n] = posP; } }
      else if (kv_at(t) >= 0) { posP += 6; endKV[kv_at(t)] = posP; }
      if (t == WARMC_T[cl]) posP += WARMC_N[cl];
    }
  }
  for (int t = T_MAIN; t < T_END; ++t)
    if (s.waitP[1][t] < s.waitP[0][t]) s.waitP[0][t] = s.waitP[1][t];
  return s;
}
constexpr Sched SC = make_sched();
constexpr bool sched_ok() {
  for (int t = 0; t < T_END; ++t) if (SC.waitP[0][t] > 63 || SC.waitP[1][t] > 63 || SC.waitA[t] > 63) return false;
  for (int m = 0; m < 80; ++m) {
    const bool ahead = m < 20 || m >= 60;
    if (SC.issW[m] >= (m >= 60 ? T_TAIL + tbar_dn(SC.consW[m] - T_TAIL - 1) : SC.consW[m] - (ahead && m != 0 ? 1 : 0))) return false;
    if ((SC.issW[m] < 0) != (m < 4)) return false;
  }
  for (int cl = 0; cl < 2; ++cl) if (SC.issKV[cl][3] < SC.kv3 || SC.issKV[cl][3] > T_MAIN || SC.issKV[cl][0] < 0 || SC.issKV[cl][1] < 3 || SC.issKV[cl][2] < 3) return false;
  return SC.consW[39] <= T_MAIN + 12 && SC.consW[39] > T_MAIN && SC.consW[59] <= T_MAIN + 30 && SC.consW[59] > T_MAIN + 17 && (SC.issKV[0][3] == T_MAIN) == (SC.issKV[1][3] == T_MAIN) && SC.locW[22] == SC.locW[17] && SC.locW[23] == SC.locW[18] && SC.issW[22] >= T_MAIN && SC.issW[23] >= T_MAIN && SC.issW[60] < T_TAIL - 1;
}
static_assert(sched_ok() && HOLD <= HOLD0 + 5, "schedule");
constexpr int locX(int j) { return j == 0 ? LDS_W + 4 * WT : j < 4 ? LDS_KV + 3 * KVT + (j - 1) * 8192 : LDS_KV + KVT + (j - 4) * 8192; }
constexpr int n_issw(int t) { int n = 0; for (int m = 4; m < 80; ++m) n += SC.issW[m] == t; return n; }
constexpr int issw_at(int t, int j) { for (int m = 4; m < 80; ++m) if (SC.issW[m] == t && j-- == 0) return m; return -1; }
constexpr int n_groups(int t) { return n_issw(t) + (kv_at(t) >= 0 ? 2 : 0); }
constexpr int first_w(int t) { for (int m = 20; m < 60; ++m) if (SC.consW[m] == t) return m; return -1; }
constexpr int count_w(int t) { int n = 0; for (int m = 20; m < 60; ++m) n += SC.consW[m] == t; return n; }

template <int N> struct IC { static constexpr int v = N; };
template <int I, int N, class F> DI void static_for(F&& f) { if constexpr (I < N) { f(IC<I>{}); static_for<I + 1, N>(f); } }

DI unsigned pk2(float a, float b) { f32x2 v = {a, b}; return __builtin_bit_cast(unsigned, __builtin_convertvector(v, bf16x2)); }
DI bf16x8 as_frag(u32x4 v) { return __builtin_bit_cast(bf16x8, v); }
DI f32x4 mfma(bf16x8 a, bf16x8 b, f32x4 c) { return __builtin_amdgcn_mfma_f32_16x16x32_bf16(a, b, c, 0, 0, 0); }
DI f32x16 mfma32(bf16x8 a, bf16x8 b, f32x16 c) { return __builtin_amdgcn_mfma_f32_32x32x16_bf16(a, b, c, 0, 0, 0); }
DI bf16x8 tr_frag(LAS char* lo, LAS char* hi) {
  s16x4 a = __builtin_amdgcn_ds_read_tr16_b64_v4i16((LAS s16x4*)lo);
  s16x4 b = __builtin_amdgcn_ds_read_tr16_b64_v4i16((LAS s16x4*)hi);
  return __builtin_bit_cast(bf16x8, __builtin_shufflevector(a, b, 0, 1, 2, 3, 4, 5, 6, 7));
}
DI LAS char* rebase(LAS char* p, int off) {
  unsigned a = (unsigned)(uintptr_t)p + off;
  asm volatile("" : "+v"(a));
  return (LAS char*)(uintptr_t)a;
}
DI int swz(int k) { return ((k & 3) << 1) ^ ((k >> 2) * 5); }
DI int swk(int k) { return ((k & 1) << 2) | (k >> 1); }
DI float max3(float a, float b, float c) { return __builtin_elementwise_maximum(__builtin_elementwise_maximum(a, b), c); }
DI float hswap_max(float v) {
  auto r = __builtin_amdgcn_permlane32_swap(__float_as_uint(v), __float_as_uint(v), false, false);
  return __builtin_elementwise_maximum(__uint_as_float(r[0]), __uint_as_float(r[1]));
}
DI float hswap_sum(float v) {
  auto r = __builtin_amdgcn_permlane32_swap(__float_as_uint(v), __float_as_uint(v), false, false);
  return __uint_as_float(r[0]) + __uint_as_float(r[1]);
}
DI u32x4 pk8(const f32x16& a, int o) { return u32x4{pk2(a[o], a[o + 1]), pk2(a[o + 2], a[o + 3]), pk2(a[o + 4], a[o + 5]), pk2(a[o + 6], a[o + 7])}; }
template <int N> DI void bar_v() {
  if constexpr (N >= 0) asm volatile("s_waitcnt vmcnt(%0) lgkmcnt(0)\n\ts_barrier" ::"i"(N) : "memory");
  else asm volatile("s_waitcnt lgkmcnt(0)\n\ts_barrier" ::: "memory");
}
DI unsigned ld_dev(const unsigned* p) { return __hip_atomic_load((const GAS unsigned*)p, __ATOMIC_RELAXED, __HIP_MEMORY_SCOPE_AGENT); }
DI void st_dev(unsigned* p, unsigned v) { __hip_atomic_store((GAS unsigned*)p, v, __ATOMIC_RELAXED, __HIP_MEMORY_SCOPE_AGENT); }
typedef __amdgpu_buffer_rsrc_t srd_t;
#define DMA3_ASM(POL)                                                                                          \
  asm volatile("s_mov_b32 m0, %0\n\ts_nop 4\n\t"                                                               \
               "buffer_load_dwordx4 %3, %1, %2 offen" POL " lds\n\t"                                           \
               "buffer_load_dwordx4 %4, %1, %2 offen offset:1024" POL " lds\n\t"                               \
               "buffer_load_dwordx4 %5, %1, %2 offen offset:2048" POL " lds" ::"s"(m0v),                       \
               "s"(srd), "s"(soff), "v"(v0), "v"(v1), "v"(v2) : "memory")
#define DMA1_ASM(POL)                                                                                          \
  asm volatile("s_mov_b32 m0, %0\n\ts_nop 4\n\t"                                                               \
               "buffer_load_dwordx4 %3, %1, %2 offen" POL " lds" ::"s"(m0v), "s"(srd), "s"(soff), "v"(v0) : "memory")
DI void dma3_kv(unsigned m0v, srd_t srd, unsigned soff, int v0, int v1, int v2) { DMA3_ASM(POL_KV); }
DI void dma3_w(unsigned m0v, srd_t srd, unsigned soff, int v0, int v1, int v2) { DMA3_ASM(POL_W); }
DI void dma1_x(unsigned m0v, srd_t srd, unsigned soff, int v0) { DMA1_ASM(POL_X); }
DI void dma1_warm(unsigned m0v, srd_t srd, unsigned soff, int v0) { DMA1_ASM(POL_WARM); }

struct NoHook { DI void operator()() const {} };
template <int N, int D, class LD, class US, class PRE = NoHook> DI void stream(LD&& ld, US&& us, PRE&& pre = PRE{}) {
  bf16x8 f[D];
  static_for<0, D>([&](auto I) { f[decltype(I)::v] = ld(I); });
  pre();
  static_for<0, N>([&](auto I) {
    constexpr int i = decltype(I)::v;
    const bf16x8 cur = f[i % D];
    if constexpr (i + D < N) f[i % D] = ld(IC<i + D>{});
    __builtin_amdgcn_sched_barrier(0x6);
    us(I, cur);
    __builtin_amdgcn_sched_barrier(0x6);
  });
}
template <int D> DI void bar_in() { asm volatile("s_waitcnt lgkmcnt(%0)\n\ts_barrier" ::"i"(2 * D) : "memory"); }
__global__ __launch_bounds__(512, 2) void fused_attn(const uint16_t* __restrict__ x, const uint16_t* __restrict__ kc,
                                                     const uint16_t* __restrict__ vc, const uint16_t* __restrict__ wq,
                                                     const uint16_t* __restrict__ wo, uint16_t* __restrict__ part,
                                                     unsigned* __restrict__ flags, uint16_t* __restrict__ out
) {
  __shared__ __attribute__((aligned(1024))) char smem[LDS_BYTES + 4096];
  LAS char* L = (LAS char*)smem;
  const unsigned Lb = (unsigned)(uintptr_t)L;
  const int wave = __builtin_amdgcn_readfirstlane(threadIdx.x >> 6), lane = threadIdx.x & 63;
  const int w = wave & 3;
  const int xcd = blockIdx.x & 7, idx = blockIdx.x >> 3, b = idx >> 1, hp = idx & 1;
  const int h1 = xcd * 4 + hp * 2;
  const int rot = 2 * ((b * 10) >> 4);
  const int g = lane >> 4;
  uint64_t pc;
  asm volatile("s_getpc_b64 %0" : "=s"(pc));
  const srd_t srdWq = __builtin_amdgcn_make_buffer_rsrc((void*)wq, 0, DM * NH * DH * 2, 0x00020000);
  const srd_t srdWo = __builtin_amdgcn_make_buffer_rsrc((void*)wo, 0, NH * DH * DM * 2, 0x00020000);
  asm volatile("" ::"s"(x), "s"(kc), "s"(vc), "s"(wq), "s"(wo), "s"(part), "s"(flags), "s"(out));
  {
    const srd_t srdX = __builtin_amdgcn_make_buffer_rsrc((void*)x, 0, NB * SQ * DM * 2, 0x00020000);
    const int vx = (8 * wave + (lane >> 3)) * 1280 + (((lane & 7) ^ (4 * (wave & 1) + g)) << 4);
    static_for<0, 10>([&](auto J) {
      constexpr int j = decltype(J)::v;
      int jt = j + (rot >> 1); jt -= jt >= 10 ? 10 : 0;
      dma1_x(Lb + locX(j) + wave * 1024, srdX, b * (SQ * DM * 2) + jt * 128, vx);
    });
  }
  if (wave < 2) {
    const srd_t srdC = __builtin_amdgcn_make_buffer_rsrc((void*)(pc & ~(uint64_t)255), 0, CODE_PF, 0x00020000);
    dma1_warm(Lb + WARM_LDS, srdC, idx * 2048 + wave * 1024, lane * 16);
  }
  int vWo[3];
#pragma unroll
  for (int i = 0; i < 3; ++i) {
    const int r = 24 * w + 8 * i + (lane >> 3), c = lane & 7;
    vWo[i] = r * 1280 + ((c ^ (((r >> 1) & 1) << 2)) << 4) - 1024 * i;
  }
  auto issue_wo = [&](int m, int loc) {
    const int hh = h1 + m / 20, cg = (m % 20) >> 1, hk = m & 1;
    const unsigned soff = (hh * DH + hk * 96) * 1280 + cg * 128;
    dma3_w(Lb + loc + w * 3072, srdWo, soff, vWo[0], vWo[1], vWo[2]);
  };
  const srd_t srdP = __builtin_amdgcn_make_buffer_rsrc((void*)part, 0, NB * 16 * SQ * DM * 2, 0x00020000);
  const int me = xcd * 2 + hp;
  const int xuq = w >> 1 ? SC.xq[1] : SC.xq[0];
  const int xu0 = w >> 1 ? SC.locW[18] : SC.locW[17], xu1 = w >> 1 ? SC.xch1[1] : SC.xch1[0], xu2 = LDS_KV + (w >> 1) * WT;

  const int r16 = lane & 15;
  LAS char* tb[4];
  { const int row = 4 * g + (r16 >> 2), p = lane & 3, mm = swz(row >> 1);
#pragma unroll
    for (int e = 0; e < 4; ++e) tb[e] = L + row * 384 + ((((2 * e) ^ (mm & 6)) | ((p >> 1) ^ (mm & 1))) << 4) + 8 * (p & 1); }

  unsigned* const req = flags;
  unsigned* const ack = flags + NB * 16 * 32;

  unsigned one_lo = 0x00003f80u, one_hi = 0x3f800000u;
  asm volatile("" : "+s"(one_lo), "+s"(one_hi));
  auto add_lo = [&](unsigned d, float c) { return __builtin_amdgcn_fdot2_f32_bf16(__builtin_bit_cast(bf16x2, d), __builtin_bit_cast(bf16x2, one_lo), c, false); };
  auto add_hi = [&](unsigned d, float c) { return __builtin_amdgcn_fdot2_f32_bf16(__builtin_bit_cast(bf16x2, d), __builtin_bit_cast(bf16x2, one_hi), c, false); };
  auto final_sum = [&]() __attribute__((always_inline)) {
    const int fr = me * 5 + wave;
    const srd_t so = __builtin_amdgcn_make_buffer_rsrc((void*)out, 0, NB * SQ * DM * 2, 0x00020000);
    u32x4 v[16];
#pragma unroll
    for (int j = 0; j < 16; ++j) v[j] = __builtin_amdgcn_raw_buffer_load_b128(srdP, lane * 16, ((b * 16 + j) * 80 + fr) * 1024, 16);
    float s[8] = {};
#pragma unroll
    for (int j = 0; j < 16; ++j)
#pragma unroll
      for (int k = 0; k < 4; ++k) { s[2 * k] = add_lo(v[j][k], s[2 * k]); s[2 * k + 1] = add_hi(v[j][k], s[2 * k + 1]); }
    const int oo = (b * SQ + ((fr >> 2) & 1) * 32) * DM + (fr >> 3) * 64 + ((fr >> 1) & 1) * 32 + (fr & 1) * 16;
    const auto s0 = __builtin_amdgcn_permlane32_swap(pk2(s[0], s[1]), pk2(s[4], s[5]), false, false), s1 = __builtin_amdgcn_permlane32_swap(pk2(s[2], s[3]), pk2(s[6], s[7]), false, false);
    __builtin_amdgcn_raw_buffer_store_b128(u32x4{s0[0], s1[0], s0[1], s1[1]}, so, (lane & 31) * (DM * 2) + 16 * (lane >> 5), oo * 2, 0);
  };
  if (wave < 4) {
    const int hh = lane >> 5, r32 = lane & 31, q4 = r16 >> 2, xh = w & 1;
    LAS char* kb[4];
#pragma unroll
    for (int j = 0; j < 4; ++j) kb[j] = L + r32 * 384 + (((2 * j) ^ hh ^ swk((r32 >> 1) & 7)) << 4);
    unsigned tag = 0;
    if (wave == 0) tag = ld_dev(ack + (b * 16 + me) * 32 + r16);

    LAS char* c4[4];
    u32x4 q[12];
    auto read_q = [&](int loc) {
#pragma unroll
      for (int j = 0; j < 4; ++j) c4[j] = rebase(kb[j], loc);
#pragma unroll
      for (int d = 0; d < 12; ++d) q[d] = *(LAS u32x4*)(c4[d & 3] + (d >> 2) * 128);
    };
    LAS char *vb[8], *c8[8];
    f32x16 o[6];
    auto zero_o = [&](int i) {
#pragma unroll
      for (int j = 0; j < 16; ++j) o[i][j] = 0.f;
      asm volatile("" : "+v"(o[i]));
    };
    {
      f32x16 qa[3];
      bf16x8 xf[40];
#pragma unroll
      for (int i = 0; i < 3; ++i)
#pragma unroll
        for (int j = 0; j < 16; ++j) qa[i][j] = 0.f;
      LAS char* wb[2];
#pragma unroll
      for (int e = 0; e < 2; ++e)
        wb[e] = L + (4 * hh + q4) * 384 + (w & 1) * (128 << e) + (((4 * (e ^ (w & 1)) + 2 * (g & 1) + ((lane & 3) >> 1)) ^ ((q4 >> 1) << 2)) << 4) + 8 * (lane & 1);
      LAS char* xa[4];
#pragma unroll
      for (int ks = 0; ks < 4; ++ks) xa[ks] = rebase(L, locX(0) + (32 * (w >> 1) + r32) * 128 + (((2 * ks + hh) ^ ((r32 >> 1) & 7)) << 4));
      bar_v<0>();
#pragma unroll
      for (int i = 0; i < 4; ++i) { xf[i] = *(LAS bf16x8*)xa[i]; xa[i] = rebase(xa[i], locX(4) - locX(0)); }
      stream<120, 4>(
          [&](auto I) -> bf16x8 {
            constexpr int i = decltype(I)::v, j = i % 6, t = j % 3;
            if constexpr (j < 2) c4[j] = rebase(wb[j], SC.locW[i / 6]);
            LAS char* p = c4[t & 1] + (t >> 1) * 128 + (j / 3) * (8 * 384);
            return tr_frag(p, p + 6144);
          },
          [&](auto I, bf16x8 f) {
            constexpr int i = decltype(I)::v;
            qa[i % 3] = mfma32(f, xf[2 * (i / 6) + (i % 6) / 3], qa[i % 3]);
            if constexpr (i < 18 && i % 6 < 4) {
#pragma unroll
              for (int k = 4 + 12 * (i / 6) + 3 * (i % 6); k < 7 + 12 * (i / 6) + 3 * (i % 6); ++k) xf[k] = *(LAS bf16x8*)(xa[k & 3] + locX(k >> 2) - locX(4));
            }
            if constexpr (i == 5 || i == 17) bar_in<2>();
            else if constexpr (i % 6 == 5 && i < 119) bar_in<4>();
            if constexpr (i == 30) {
              ++tag;
              if (wave == 0 && lane < 16) st_dev(req + (b * 16 + lane) * 32 + me, tag);
            }
            if constexpr (i >= 66 && i < 102 && i % 6 == 0) zero_o((i - 66) / 6);
            if constexpr (i == 102 || i == 108) {
              int lz = lane;
              asm volatile("" : "+v"(lz));
              const int h2 = lz >> 5, q2 = (lz & 15) >> 2, r2 = lz & 31;
#pragma unroll
              for (int j = 0; j < 8; ++j) {
                if constexpr (i == 102) {
                  const int hi = j >> 2, e = j & 3, mm = ((q2 >> 1) << 2) | (2 * hi + h2);
                  const int line = e == 0 ? 1 - xh : e == 1 ? 2 - 2 * xh : e == 2 ? xh : 2 * xh, half = e == 0 || e == 3 ? 1 - xh : xh;
                  vb[j] = rebase(L, (32 * xh + 8 * hi + 4 * h2 + q2) * 384 + line * 128 + (((4 * half + 2 * ((lz >> 4) & 1) + ((lz & 3) >> 1)) ^ mm) << 4) + 8 * (lz & 1));
                } else {
                  const int e = j >> 2;
                  c8[j] = rebase(L, xu0 + r2 * 384 + xh * (128 << e) + (((4 * (e ^ xh) + (j & 3)) ^ swk((r2 >> 1) & 7)) << 4) + 8 * h2);
                }
              }
            }
          });
#pragma unroll
      for (int t = 0; t < 3; ++t)
#pragma unroll
        for (int i = 0; i < 4; ++i)
          *(LAS u32x2*)(c8[4 * (t & 1) + i] + (t >> 1) * 128) = u32x2{pk2(qa[t][4 * i], qa[t][4 * i + 1]), pk2(qa[t][4 * i + 2], qa[t][4 * i + 3])};
      bar_v<-1>();
      read_q(xu0);
    }

    auto v_ld = [&](auto T_, auto S_) -> bf16x8 {
      constexpr int t = decltype(T_)::v, e = t == 0 || t == 2 ? 0 : t == 1 ? 1 : t == 4 ? 3 : 2;
      constexpr int off = (t == 2 || t == 5 ? 128 : 0) + decltype(S_)::v * 6144;
      return tr_frag(c8[e] + off, c8[4 + e] + off);
    };
#pragma nounroll
    for (int p = 0; p < 2; ++p) {
      float mrun = -__builtin_inff(), lsum = 0.f;
      bf16x8 pf[2];
      auto k_interval = [&](int c, auto LAST) {
        const int ks = LDS_KV + (c & 1) * 2 * KVT;
        bar_v<-1>();
        f32x16 s;
#pragma unroll
        for (int j = 0; j < 16; ++j) s[j] = 0.f;
#pragma unroll
        for (int j = 0; j < 4; ++j) c4[j] = rebase(kb[j], ks + xh * WT);
        stream<12, 6>(
            [&](auto I) -> bf16x8 { constexpr int i = decltype(I)::v; return *(LAS bf16x8*)(c4[i & 3] + (i >> 2) * 128); },
            [&](auto I, bf16x8 f) { constexpr int i = decltype(I)::v; s = mfma32(f, as_frag(q[i]), s); });
        if constexpr (decltype(LAST)::v) if (p == 0) read_q(xuq);
        float mx = max3(max3(s[0], s[1], s[2]), max3(s[3], s[4], s[5]), max3(s[6], s[7], s[8]));
        mx = max3(max3(mx, s[9], s[10]), max3(s[11], s[12], s[13]), max3(s[14], s[15], s[15]));
        mx = hswap_max(mx);
        if (__any(mx > mrun + LAZY_THR / SL2E)) {
          const float mnew = fmaxf(mrun, mx), alpha = __builtin_amdgcn_exp2f((mrun - mnew) * SL2E);
          mrun = mnew;
          lsum *= alpha;
#pragma unroll
          for (int i = decltype(LAST)::v ? 3 : 0; i < 6; ++i) o[i] *= alpha;
        }
        const float mb = -mrun * SL2E;
#pragma unroll
        for (int j = 0; j < 16; ++j) s[j] = __builtin_amdgcn_exp2f(fmaf(s[j], SL2E, mb));
        lsum += ((s[0] + s[1]) + (s[2] + s[3])) + ((s[4] + s[5]) + (s[6] + s[7])) + (((s[8] + s[9]) + (s[10] + s[11])) + ((s[12] + s[13]) + (s[14] + s[15])));
        pf[0] = as_frag(pk8(s, 0)); pf[1] = as_frag(pk8(s, 8));
#pragma unroll
        for (int j = 0; j < 8; ++j) c8[j] = rebase(vb[j], ks + KVT);
        __builtin_amdgcn_sched_barrier(0);
      };
#pragma nounroll
      for (int c = 0; c < 7; ++c) {
        k_interval(c, IC<0>{});
        bar_v<-1>();
        stream<12, 5>([&](auto I) -> bf16x8 { constexpr int i = decltype(I)::v; return v_ld(IC<i % 6>{}, IC<i / 6>{}); },
                      [&](auto I, bf16x8 f) { constexpr int i = decltype(I)::v; o[i % 6] = mfma32(f, pf[i / 6], o[i % 6]); });
      }
      const int xu = p == 0 ? xu1 : xu2;
      LAS char* const mine = rebase(L, xu + xh * 6144 + lane * 16);
      LAS char* const peer = rebase(L, xu + (xh ^ 1) * 6144 + lane * 16);
#pragma unroll
      for (int i = 0; i < 6; ++i) *(LAS u32x4*)(mine + i * 1024) = pk8(o[i >> 1], 8 * (i & 1));
      *(LAS float*)(L + MS_LDS + w * 128 + r32 * 4) = mrun;
      k_interval(7, IC<1>{});
      LAS char* const px = rebase(L, (p == 0 ? SC.xp1 : 2 * WT) + lane * 16);
      *(LAS bf16x8*)(px + w * 2048) = pf[0];
      *(LAS bf16x8*)(px + w * 2048 + 1024) = pf[1];
      lsum = hswap_sum(lsum);
      *(LAS f32x2*)(L + ML_LDS + w * 256 + r32 * 8) = f32x2{mrun, lsum};
#pragma unroll
      for (int j = 0; j < 2; ++j) { c8[j] = rebase(c8[2 + j], (1 - 2 * xh) * WT); c8[4 + j] = rebase(c8[6 + j], (1 - 2 * xh) * WT); }
      bar_v<-1>();
      f32x2 ml;
      float ms, a0, a1;
      bf16x8 pq[2];
      u32x4 r[6];
      stream<12, 5>(
          [&](auto I) -> bf16x8 {
            constexpr int i = decltype(I)::v, t = (i % 6) >> 1, e = (i < 6 ? 2 : 0) + (t == 1), off = (t == 2 ? 128 : 0) + (i & 1) * 6144;
            return tr_frag(c8[e] + off, c8[4 + e] + off);
          },
          [&](auto I, bf16x8 f) {
            constexpr int i = decltype(I)::v, t = (i % 6) >> 1;
            if constexpr (i < 6) o[3 + t] = mfma32(f, pf[i & 1], o[3 + t]);
            else o[t] = mfma32(f, pq[i & 1], o[t]);
            if constexpr (i == 0) {
#pragma unroll
              for (int k = 0; k < 2; ++k) r[k] = *(LAS u32x4*)(peer + k * 1024);
              ml = *(LAS f32x2*)(L + ML_LDS + (w ^ 1) * 256 + r32 * 8);
              ms = *(LAS float*)(L + MS_LDS + (w ^ 1) * 128 + r32 * 4);
            }
            if constexpr (i == 1 || i == 2) {
#pragma unroll
              for (int k = 0; k < 2; ++k) r[2 * i + k] = *(LAS u32x4*)(peer + (2 * i + k) * 1024);
            }
            if constexpr (i == 3) { pq[0] = *(LAS bf16x8*)(px + (w ^ 1) * 2048); pq[1] = *(LAS bf16x8*)(px + (w ^ 1) * 2048 + 1024); }
            if constexpr (i >= 2 && i < 5) {
#pragma unroll
              for (int k = 0; k < 8; ++k) {
                const unsigned d = r[2 * (i - 2) + (k >> 2)][k & 3];
                o[i - 2][2 * k] = __uint_as_float(d << 16); o[i - 2][2 * k + 1] = __uint_as_float(d & 0xffff0000u);
              }
            }
            if constexpr (i == 5) {
              if (__any(ms != ml[0])) {
                const float f2 = __builtin_amdgcn_exp2f((ms - ml[0]) * SL2E);
#pragma unroll
                for (int k = 0; k < 3; ++k) o[k] *= f2;
              }
              const float mt = fmaxf(mrun, ml[0]);
              a0 = __builtin_amdgcn_exp2f((mrun - mt) * SL2E); a1 = __builtin_amdgcn_exp2f((ml[0] - mt) * SL2E);
              const float inv = __builtin_amdgcn_rcpf(lsum * a0 + ml[1] * a1);
              a0 *= inv; a1 *= inv;
            }
            if constexpr (i >= 6 && i < 9) o[i - 3] *= a0;
            if constexpr (i == 9 || i == 11) {
              constexpr int k = (i - 9) >> 1;
              o[k] = o[k] * a1 + o[3 + k];
              *(LAS u32x4*)(peer + 2 * k * 1024) = pk8(o[k], 0);
              *(LAS u32x4*)(peer + (2 * k + 1) * 1024) = pk8(o[k], 8);
            }
          });
      o[2] = o[2] * a1 + o[5];
      *(LAS u32x4*)(peer + 4096) = pk8(o[2], 0);
      *(LAS u32x4*)(peer + 5120) = pk8(o[2], 8);
      if (p == 0)
#pragma unroll
        for (int i = 0; i < 6; ++i) zero_o(i);
    }
    static_for<T_TAIL, T_END>([&](auto T) {
      constexpr int t = decltype(T)::v;
      if constexpr (tbar(t - T_TAIL)) {
        bar_v<SC.waitA[t]>();
        static_for<60, 80>([&](auto M) { constexpr int m = decltype(M)::v; if constexpr (SC.issW[m] == t) {
          issue_wo(m - 40, SC.locW[m]);
          if constexpr (A_PACE > 0 && SC.issW[m < 79 ? m + 1 : m] == t && m < 79) __builtin_amdgcn_s_sleep(A_PACE);
        } });
      }
    });
    unsigned seen = 0;
    if (wave == 0) seen = ld_dev(req + (b * 16 + me) * 32 + r16);
    bar_v<0>();
    if (wave == 0) {
      if (lane < 16) st_dev(ack + (b * 16 + lane) * 32 + me, seen);
#pragma nounroll
      for (int spin = 0; spin < (1 << 18); ++spin) {
        const unsigned a = ld_dev(ack + (b * 16 + me) * 32 + r16);
        if (__all(a == tag)) break;
        __builtin_amdgcn_s_sleep(1);
      }
    }
    bar_v<-1>();
    final_sum();
    if (wave == 0) {
      const unsigned r = ld_dev(req + (b * 16 + me) * 32 + r16);
      if (r != seen && lane < 16) st_dev(ack + (b * 16 + lane) * 32 + me, r);
    }
    __builtin_amdgcn_endpgm();
  }
  {
    __builtin_amdgcn_s_setprio(PRIO_P);
    const srd_t srdK = __builtin_amdgcn_make_buffer_rsrc((void*)kc, 0, NB * NH * SK * DH * 2, 0x00020000);
    const srd_t srdV = __builtin_amdgcn_make_buffer_rsrc((void*)vc, 0, NB * NH * SK * DH * 2, 0x00020000);
    int vKV[6], vWq[3], vW1[3];
#pragma unroll
    for (int i = 0; i < 3; ++i) {
      const int p = 192 * w + 64 * i + lane, r = p / 24, c = p - 24 * r;
      const int kr = 8 * ((r >> 2) & 3) + 4 * (r >> 4) + (r & 3);
      vWq[i] = kr * 12288 + ((c ^ swz((r >> 1) & 7)) << 4) - 1024 * i;
      vW1[i] = kr * 12288 + ((c ^ (((r >> 1) & 1) << 2)) << 4) - 1024 * i;
    }
    auto issue_w = [&](auto M) {
      constexpr int m = decltype(M)::v;
      if constexpr (m < 40) {
        int kt = m % 20 + rot; kt -= kt >= 20 ? 20 : 0;
        const unsigned soff = kt * (32 * 12288) + (h1 + m / 20) * 384;
        if constexpr (m < 20) dma3_w(Lb + SC.locW[m] + w * 3072, srdWq, soff, vW1[0], vW1[1], vW1[2]);
        else dma3_w(Lb + SC.locW[m] + w * 3072, srdWq, soff, vWq[0], vWq[1], vWq[2]);
      } else issue_wo(m - 40, SC.locW[m]);
    };
    static_for<0, 4>([&](auto M) { issue_w(M); });
#pragma unroll
    for (int i = 0; i < 6; ++i) {
      const int p = 64 * i + lane, r = p / 24;
      vKV[i] = ((16 * lane) ^ (swk(r >> 1) << 4)) + (i >= 3 ? 3072 : 0);
    }
    const unsigned kv0 = (unsigned)(b * NH + h1) * (SK * DH * 2) + w * 6144;
    auto issue_kv_half = [&](auto N, auto H) {
      constexpr int n = decltype(N)::v, hf = decltype(H)::v;
      const unsigned soff = kv0 + (n >> 4) * (SK * DH * 2) + ((n & 15) >> 1) * KVT, m0v = Lb + LDS_KV + (n & 3) * KVT + w * 6144;
      dma3_kv(m0v + hf * 3072, n & 1 ? srdV : srdK, soff, vKV[3 * hf], vKV[3 * hf + 1], vKV[3 * hf + 2]);
    };
    auto issue_kv = [&](auto N) { issue_kv_half(N, IC<0>{}); issue_kv_half(N, IC<1>{}); };
    bf16x8 xf[20];
    auto read_x = [&]() {
      LAS char* const x0 = L + (16 * w + r16) * 128 + ((g ^ (r16 >> 1)) << 4);
      LAS char* const x1 = L + (16 * w + r16) * 128 + (((4 + g) ^ (r16 >> 1)) << 4);
      LAS char* const xa[2] = {rebase(x0, locX(0)), rebase(x1, locX(0))};
      LAS char* const xb[2] = {rebase(x0, locX(4)), rebase(x1, locX(4))};
#pragma unroll
      for (int s = 0; s < 20; ++s) xf[s] = s < 2 ? *(LAS bf16x8*)xa[s] : *(LAS bf16x8*)(xb[s & 1] + locX(s >> 1) - locX(4));
    };
    const int hh = lane >> 5, q4 = r16 >> 2;
    LAS char* const ob = L + (4 * hh + q4) * 128 + (((4 * (w & 1) + 2 * (g & 1) + ((lane & 3) >> 1)) ^ ((q4 >> 1) << 2)) << 4) + 8 * (lane & 1);
    f32x16 acc[10];
    f32x4 qa[12];
#pragma unroll
    for (int i = 0; i < 10; ++i)
#pragma unroll
      for (int j = 0; j < 16; ++j) acc[i][j] = 0.f;
#pragma unroll
    for (int i = 0; i < 12; ++i) qa[i] = f32x4{0.f, 0.f, 0.f, 0.f};
    bf16x8 of[12];
    LAS char* c4[4];
    const unsigned pbase = (unsigned)((b * 16 + me) * 10) * 8192 + w * 2048 + lane * 16;
    auto read_o = [&](int unit) {
      LAS char* const xb = rebase(L, unit + lane * 16);
#pragma unroll
      for (int s = 0; s < 12; ++s) of[s] = *(LAS bf16x8*)(xb + (s < 6 ? 6144 : -6144) + s * 1024);
    };
    auto wo_ld = [&](auto M, auto J) -> bf16x8 {
      constexpr int m = decltype(M)::v, j = decltype(J)::v;
      if constexpr (j == 0) c4[0] = rebase(ob, SC.locW[m]);
      LAS char* p = c4[0] + j * 2048;
      return tr_frag(p, p + 1024);
    };
    auto startup = [&](auto CL) {
      constexpr int cl = decltype(CL)::v;
      static_for<0, T_MAIN>([&](auto T) {
        constexpr int t = decltype(T)::v;
        bar_v<SC.waitP[cl][t]>();
        constexpr bool kv_first = cl == 0 && t == HOLD;
        if constexpr (kv_first) static_for<0, 4>([&](auto N) { if constexpr (SC.issKV[cl][decltype(N)::v] == t) issue_kv(N); });
        static_for<4, 40>([&](auto M) {
          constexpr int i0 = SC.issW[decltype(M)::v];
          if constexpr ((cl == 0 && i0 > HOLD0 && i0 < HOLD ? HOLD : i0) == t) issue_w(M);
        });
        if constexpr (!kv_first) static_for<0, 4>([&](auto N) { if constexpr (SC.issKV[cl][decltype(N)::v] == t) issue_kv(N); });
        if constexpr (t == 0) read_x();
        if constexpr (t == WARMC_T[cl]) {
#pragma unroll
          for (int k = 0; k < WARMC_N[cl]; ++k)
            dma1_warm(Lb + WARM_LDS, srdWo, (h1 * DH + 48 * (b >> 1)) * 1280 + (((cl ? WARMC_N[0] : 0) + k) * 4 + w) * 1024, lane * 16);
        }
      });
    };
    if (CLASS_EXPR) startup(IC<1>{}); else startup(IC<0>{});
    bar_v<-1>();
    static_for<T_MAIN, T_TAIL>([&](auto T) {
      constexpr int t = decltype(T)::v;
      bar_v<SC.waitP[0][t]>();
      if constexpr (t == T_MAIN + 13) {
        const int mq = swk(r16 >> 1) ^ (g >> 1);
        LAS char* const qw = rebase(L, xuq + (16 * (w & 1) + r16) * 384 + 8 * (g & 1));
#pragma unroll
        for (int dt = 0; dt < 12; ++dt)
          *(LAS u32x2*)(qw + (dt >> 2) * 128 + (((2 * (dt & 3)) ^ mq) << 4)) = u32x2{pk2(qa[dt][0], qa[dt][1]), pk2(qa[dt][2], qa[dt][3])};
      }
      if constexpr (t == T_MAIN + 16) read_o(xu1);
      auto group = [&](auto J) {
        constexpr int j = decltype(J)::v;
        if constexpr (j < n_issw(t)) issue_w(IC<issw_at(t, j)>{});
        else issue_kv_half(IC<kv_at(t)>{}, IC<j - n_issw(t)>{});
      };
      auto groups_at = [&](auto I, auto N) {
        if constexpr (ILV) static_for<0, n_groups(t)>([&](auto J) { if constexpr (decltype(J)::v * decltype(N)::v / n_groups(t) == decltype(I)::v) group(J); });
      };
      if constexpr (count_w(t) > 0) {
        constexpr int m0 = first_w(t);
        if constexpr (m0 < 40)
          stream<12 * count_w(t), 7>(
              [&](auto I) -> bf16x8 {
                constexpr int i = decltype(I)::v, j = i % 12;
                if constexpr (j < 4) c4[j] = rebase(tb[j], SC.locW[m0 + i / 12]);
                LAS char* p = c4[j & 3] + (j >> 2) * 128;
                return tr_frag(p, p + 6144);
              },
              [&](auto I, bf16x8 f) {
                constexpr int i = decltype(I)::v;
                qa[i % 12] = mfma(f, xf[m0 - 20 + i / 12], qa[i % 12]);
                groups_at(I, IC<12 * count_w(t)>{});
              });
        else
          stream<6 * count_w(t), 4>(
              [&](auto I) -> bf16x8 { constexpr int i = decltype(I)::v; return wo_ld(IC<m0 + i / 6>{}, IC<i % 6>{}); },
              [&](auto I, bf16x8 f) {
                constexpr int i = decltype(I)::v, m = m0 - 40 + i / 6;
                acc[m >> 1] = mfma32(f, of[6 * (m & 1) + i % 6], acc[m >> 1]);
                groups_at(I, IC<6 * count_w(t)>{});
              });
        __builtin_amdgcn_sched_barrier(0);
      }
      if constexpr (!ILV || count_w(t) == 0) static_for<0, n_groups(t)>(group);
    });
    u32x4 sv0, sv1;
    auto store_step = [&](auto C, auto K, u32x4& sv) {
      constexpr int cg = decltype(C)::v, k = decltype(K)::v;
      sv[k & 3] = pk2(acc[cg][2 * k], acc[cg][2 * k + 1]);
      if constexpr ((k & 3) == 3) __builtin_amdgcn_raw_buffer_store_b128(sv, srdP, pbase, cg * 8192 + (k >> 2) * 1024, 16);
    };
    stream<120, 4>([&](auto I) -> bf16x8 { constexpr int i = decltype(I)::v; return wo_ld(IC<60 + i / 6>{}, IC<i % 6>{}); },
                   [&](auto I, bf16x8 f) {
                     constexpr int i = decltype(I)::v, cg = i / 12, j = i % 12;
                     acc[cg] = mfma32(f, of[j], acc[cg]);
                     if constexpr (j == 11 && tbar(cg + 1)) bar_in<4>();
                     if constexpr (j >= 2 && j < 10 && cg > SDEFER) store_step(IC<cg - 1>{}, IC<j - 2>{}, sv0);
                     if constexpr (j >= 4 && cg > SDEFER && cg - SDEFER - 1 < SDEFER) store_step(IC<cg - SDEFER - 1>{}, IC<j - 4>{}, sv1);
                     if constexpr (j >= 2 && cg > SDEFER) __builtin_amdgcn_sched_barrier(0);
                   },
                   [&]() {
                     bar_v<SC.waitP[0][T_TAIL]>();
                     read_o(xu2);
                   });
    static_for<0, 8>([&](auto K) { store_step(IC<9>{}, K, sv0); });
    bar_v<0>();
    bar_v<-1>();
    if (wave == 4) final_sum();
  }
}

}

extern "C" void kernel_launch(void* const* d_in, const int* in_sizes, int n_in, void* d_out, int out_size, void* d_ws,
                              size_t ws_size, hipStream_t stream) {
  const uint16_t* x = (const uint16_t*)d_in[0];
  const uint16_t* kc = (const uint16_t*)d_in[1];
  const uint16_t* vc = (const uint16_t*)d_in[2];
  const uint16_t* wq = (const uint16_t*)d_in[3];
  const uint16_t* wo = (const uint16_t*)d_in[4];
  uint16_t* part = (uint16_t*)d_ws;
  unsigned* flags = (unsigned*)((char*)d_ws + (32 << 20));
  fused_attn<<<256, 512, 0, stream>>>(x, kc, vc, wq, wo, part, flags, (uint16_t*)d_out);
}
```
